```python
import torch

def _softmax(x, axis=-1):
    return torch.softmax(x, dim=axis)
def _gelu(x):
    return torch.nn.functional.gelu(x, approximate="tanh")
def _silu(x):
    return torch.nn.functional.silu(x)
def _sigmoid(x):
    return torch.sigmoid(x)
def _layernorm(x, axis=-1, eps=1e-5):
    m = x.mean(dim=axis, keepdim=True)
    v = x.var(dim=axis, keepdim=True, unbiased=False)
    return (x - m) / torch.sqrt(v + eps)
def _scatter_add(tgt, idx, src, axis=0):
    return tgt.clone().index_add_(axis, idx, src)
def _segment_sum(src, seg, n):
    out = torch.zeros((n,) + src.shape[1:], dtype=src.dtype)
    return out.index_add_(0, seg, src)
def _pad(x, pw):
    flat = tuple(v for pair in reversed(pw) for v in pair)
    return torch.nn.functional.pad(x, flat)

kernel_name = 'standalone__paged_block_table_kv_pagedattention__sampling'

def setup_inputs():

    _w = (lambda a: (lambda t: t.to(torch.bfloat16) if t.is_floating_point() else t)(torch.as_tensor(a)))
    B, Hq, Hkv, Sq, Skv, D = 16, 32, 16, 16, 4096, 128
    bs = 16
    nbps = max(1, Skv // bs)
    num_phys = B * nbps + 4
    bt = torch.randperm(num_phys)[: B * nbps].reshape(B, nbps).to(torch.int64)
    cl = (nbps * bs - (torch.arange(1, B + 1, dtype=torch.int64) * 2654435761) % max(1, nbps * bs // 2)).to(torch.int64)
    return {
        "q": _w((lambda *s: torch.randn(*s, dtype=torch.bfloat16) * 0.8)(B, Hq, Sq, D)),
        "k_cache": _w((lambda *s: torch.randn(*s, dtype=torch.bfloat16) * 0.8)(num_phys, bs, Hkv, D)),
        "v_cache": _w((lambda *s: torch.randn(*s, dtype=torch.bfloat16) * 0.05)(num_phys, bs, Hkv, D)),
        "block_table": _w(bt),
        "context_lens": _w(cl),
    }

def reference(q, k_cache, v_cache, block_table, context_lens):
    B, Hq, Sq, D = q.shape
    bs = k_cache.shape[1]
    Hkv = k_cache.shape[2]
    nbps = block_table.shape[1]
    Skv = nbps * bs
    bt = block_table.reshape(-1)
    k = (lambda x, *d: x.permute(*d))(k_cache[bt].reshape(B, Skv, Hkv, D), 0, 2, 1, 3)
    v = (lambda x, *d: x.permute(*d))(v_cache[bt].reshape(B, Skv, Hkv, D), 0, 2, 1, 3)
    g = Hq // Hkv
    qg = q.reshape(B, Hkv, g, Sq, D)
    scores = torch.einsum("bhgqd,bhkd->bhgqk", qg, k) * (D ** -0.5)
    pos = torch.arange(Skv)
    msk = pos[None, :] < context_lens[:, None]
    scores = torch.where(msk[:, None, None, None, :], scores, -1e9)
    p = _softmax(scores, axis=-1)
    out = torch.einsum("bhgqk,bhkd->bhgqd", p, v)
    return out.reshape(B, Hq, Sq, D)
```

```cpp
#include <hip/hip_runtime.h>
#include <hip/hip_bf16.h>
#include <stdint.h>

namespace {

constexpr int kB = 16, kHQ = 32, kHKV = 16, kSQ = 16, kD = 128, kPage = 16, kNBPS = 256, kSKV = kNBPS * kPage;
constexpr int kNW = 8;
constexpr int kTokBytes = kHKV * kD * 2;
constexpr int kPageBytes = kPage * kTokBytes;
#define KV_AUX 2

typedef __attribute__((ext_vector_type(8))) __bf16 bf16x8;
typedef __attribute__((ext_vector_type(2))) __bf16 bf16x2;
typedef __attribute__((ext_vector_type(4))) float f32x4;
typedef __attribute__((ext_vector_type(4))) unsigned u32x4;

__device__ __forceinline__ f32x4 mfma16(u32x4 a, u32x4 b, f32x4 c) {
  return __builtin_amdgcn_mfma_f32_16x16x32_bf16(__builtin_bit_cast(bf16x8, a), __builtin_bit_cast(bf16x8, b), c, 0, 0, 0);
}
__device__ __forceinline__ unsigned pack_bf16(float lo, float hi) {
  bf16x2 r = {(__bf16)lo, (__bf16)hi};
  return __builtin_bit_cast(unsigned, r);
}
__device__ __forceinline__ float xq_max(float x) {
  auto s = __builtin_amdgcn_permlane16_swap(__float_as_uint(x), __float_as_uint(x), false, false);
  x = fmaxf(__uint_as_float(s[0]), __uint_as_float(s[1]));
  auto t = __builtin_amdgcn_permlane32_swap(__float_as_uint(x), __float_as_uint(x), false, false);
  return fmaxf(__uint_as_float(t[0]), __uint_as_float(t[1]));
}
__device__ __forceinline__ float xq_sum(float x) {
  auto s = __builtin_amdgcn_permlane16_swap(__float_as_uint(x), __float_as_uint(x), false, false);
  x = __uint_as_float(s[0]) + __uint_as_float(s[1]);
  auto t = __builtin_amdgcn_permlane32_swap(__float_as_uint(x), __float_as_uint(x), false, false);
  return __uint_as_float(t[0]) + __uint_as_float(t[1]);
}

constexpr int kLdsBytes = kNW * 32 * kD * 4 + kNW * 32 * 2 * 4;

__global__ void __launch_bounds__(kNW * 64, 2)
paged_attn_kernel(const unsigned short* __restrict__ q, const unsigned short* __restrict__ kc,
                  const unsigned short* __restrict__ vc, const int* __restrict__ bt,
                  const int* __restrict__ cl, unsigned short* __restrict__ out, unsigned kv_bytes) {
  extern __shared__ __attribute__((aligned(16))) unsigned char smem[];
  const int bh = blockIdx.x;
  const int b = bh >> 4, h = bh & 15;
  const int tid = threadIdx.x, lane = tid & 63;
  const int wave = __builtin_amdgcn_readfirstlane(tid >> 6);
  const int g4 = lane >> 4, c16 = lane & 15;

  int btv;
  {
    const int j = lane & 31;
    const int pg = 2 * (wave + kNW * (j >> 1)) + (j & 1);
    btv = bt[b * kNBPS + pg];
  }
  int len = cl[b];
  float cscale = 0.08838834764831845f * 1.4426950408889634f;
  if (len <= 0) { len = kSKV; cscale = 0.f; }
  len = min(len, kSKV);
  const int npages = (len + kPage - 1) / kPage;
  const int ntiles = (npages + 1) >> 1;
  const int nt_w = (ntiles > wave) ? (ntiles - wave + kNW - 1) / kNW : 0;

  const int koff = c16 * kTokBytes + g4 * 16;
  const int voff = (4 * g4) * kTokBytes + c16 * 16;

  u32x4 K[2][4], V[2][4];
  auto page_base = [&](int i, int pg) -> int {
    int lp = 2 * (wave + kNW * i) + pg;
    int sel = 2 * i + pg;
    if (lp >= npages) sel = 2 * i;
    return __builtin_amdgcn_readlane(btv, sel & 31) * kPageBytes + h * (kD * 2);
  };
  auto issueK = [&](int i) {
    const __amdgpu_buffer_rsrc_t krs = __builtin_amdgcn_make_buffer_rsrc((void*)kc, 0, i < nt_w ? kv_bytes : 0u, 0x00020000);
#pragma unroll
    for (int pg = 0; pg < 2; ++pg) {
      const int base = page_base(i, pg);
#pragma unroll
      for (int s = 0; s < 4; ++s) K[pg][s] = __builtin_amdgcn_raw_buffer_load_b128(krs, koff + 64 * s, base, KV_AUX);
    }
  };
  auto issueV = [&](int i) {
    const __amdgpu_buffer_rsrc_t vrs = __builtin_amdgcn_make_buffer_rsrc((void*)vc, 0, i < nt_w ? kv_bytes : 0u, 0x00020000);
#pragma unroll
    for (int pg = 0; pg < 2; ++pg) {
      const int base = page_base(i, pg);
#pragma unroll
      for (int t = 0; t < 4; ++t) V[pg][t] = __builtin_amdgcn_raw_buffer_load_b128(vrs, voff, base + t * kTokBytes, KV_AUX);
    }
  };

  u32x4 Q[2][4];
  {
    const unsigned short* qb = q + ((size_t)(b * kHQ + 2 * h) * kSQ) * kD;
#pragma unroll
    for (int qt = 0; qt < 2; ++qt)
#pragma unroll
      for (int s = 0; s < 4; ++s)
        Q[qt][s] = *reinterpret_cast<const u32x4*>(qb + (qt * kSQ + c16) * kD + 32 * s + 8 * g4);
  }
  issueK(0); issueV(0);
  __builtin_amdgcn_sched_barrier(0);

  f32x4 acc[2][8];
#pragma unroll
  for (int qt = 0; qt < 2; ++qt)
#pragma unroll
    for (int e = 0; e < 8; ++e) acc[qt][e] = f32x4{0.f, 0.f, 0.f, 0.f};
  float m[2] = {-INFINITY, -INFINITY}, l[2] = {0.f, 0.f};

  for (int i = 0; i < nt_w; ++i) {
    f32x4 S[2][2];
#pragma unroll
    for (int pg = 0; pg < 2; ++pg)
#pragma unroll
      for (int qt = 0; qt < 2; ++qt) {
        f32x4 a = {0.f, 0.f, 0.f, 0.f};
#pragma unroll
        for (int s = 0; s < 4; ++s) a = mfma16(K[pg][s], Q[qt][s], a);
        S[pg][qt] = a;
      }
    __builtin_amdgcn_sched_barrier(0);
    issueK(i + 1);
    __builtin_amdgcn_sched_barrier(0);
    const int tok0 = 32 * (wave + kNW * i);
    if (tok0 + 32 > len) {
#pragma unroll
      for (int pg = 0; pg < 2; ++pg)
#pragma unroll
        for (int r = 0; r < 4; ++r) {
          const bool dead = tok0 + 16 * pg + 4 * g4 + r >= len;
#pragma unroll
          for (int qt = 0; qt < 2; ++qt) S[pg][qt][r] = dead ? -INFINITY : S[pg][qt][r];
        }
    }
    u32x4 P[2];
#pragma unroll
    for (int qt = 0; qt < 2; ++qt) {
      float mx = fmaxf(fmaxf(fmaxf(S[0][qt][0], S[0][qt][1]), fmaxf(S[0][qt][2], S[0][qt][3])),
                       fmaxf(fmaxf(S[1][qt][0], S[1][qt][1]), fmaxf(S[1][qt][2], S[1][qt][3])));
      mx = xq_max(mx);
      const float mn = fmaxf(m[qt], mx * cscale);
      const float alpha = __builtin_amdgcn_exp2f(m[qt] - mn);
      m[qt] = mn;
      float p[2][4];
      float ps = 0.f;
#pragma unroll
      for (int pg = 0; pg < 2; ++pg)
#pragma unroll
        for (int r = 0; r < 4; ++r) {
          p[pg][r] = __builtin_amdgcn_exp2f(fmaf(S[pg][qt][r], cscale, -mn));
          ps += p[pg][r];
        }
      l[qt] = fmaf(l[qt], alpha, ps);
      P[qt] = u32x4{pack_bf16(p[0][0], p[0][1]), pack_bf16(p[0][2], p[0][3]), pack_bf16(p[1][0], p[1][1]), pack_bf16(p[1][2], p[1][3])};
#pragma unroll
      for (int e = 0; e < 8; ++e) acc[qt][e] *= alpha;
    }
    u32x4 A[8];
#pragma unroll
    for (int e = 0; e < 8; ++e) {
      const unsigned sel = (e & 1) ? 0x07060302u : 0x05040100u;
#pragma unroll
      for (int w = 0; w < 4; ++w)
        A[e][w] = __builtin_amdgcn_perm(V[w >> 1][2 * (w & 1) + 1][e >> 1], V[w >> 1][2 * (w & 1)][e >> 1], sel);
    }
    __builtin_amdgcn_sched_barrier(0);
    issueV(i + 1);
    __builtin_amdgcn_sched_barrier(0);
#pragma unroll
    for (int e = 0; e < 8; ++e)
#pragma unroll
      for (int qt = 0; qt < 2; ++qt) acc[qt][e] = mfma16(A[e], P[qt], acc[qt][e]);
  }

  float* sO = reinterpret_cast<float*>(smem);
  float* sM = reinterpret_cast<float*>(smem + kNW * 32 * kD * 4);
  float* sL = sM + kNW * 32;
#pragma unroll
  for (int qt = 0; qt < 2; ++qt) {
    l[qt] = xq_sum(l[qt]);
    if (g4 == 0) { sM[wave * 32 + qt * 16 + c16] = m[qt]; sL[wave * 32 + qt * 16 + c16] = l[qt]; }
  }
  __syncthreads();
#pragma unroll
  for (int qt = 0; qt < 2; ++qt) {
    float mw[kNW], M = -INFINITY;
#pragma unroll
    for (int w = 0; w < kNW; ++w) { mw[w] = sM[w * 32 + qt * 16 + c16]; M = fmaxf(M, mw[w]); }
    float L = 0.f;
#pragma unroll
    for (int w = 0; w < kNW; ++w) L = fmaf(sL[w * 32 + qt * 16 + c16], __builtin_amdgcn_exp2f(mw[w] - M), L);
    const float f = __builtin_amdgcn_exp2f(m[qt] - M) / L;
    float* dst = sO + (wave * 32 + qt * 16 + c16) * kD;
#pragma unroll
    for (int r = 0; r < 4; ++r)
#pragma unroll
      for (int eh = 0; eh < 2; ++eh) {
        f32x4 v = {acc[qt][4 * eh + 0][r] * f, acc[qt][4 * eh + 1][r] * f, acc[qt][4 * eh + 2][r] * f, acc[qt][4 * eh + 3][r] * f};
        const int chunk = (8 * g4 + 2 * r + eh) ^ (c16 & 7);
        *reinterpret_cast<f32x4*>(dst + 4 * chunk) = v;
      }
  }
  __syncthreads();
  {
    const int row = tid >> 4, cc = tid & 15;
    f32x4 s0 = {0.f, 0.f, 0.f, 0.f}, s1 = {0.f, 0.f, 0.f, 0.f};
#pragma unroll
    for (int w = 0; w < kNW; ++w) {
      const float* src = sO + (w * 32 + row) * kD;
      s0 += *reinterpret_cast<const f32x4*>(src + 4 * ((2 * cc) ^ (row & 7)));
      s1 += *reinterpret_cast<const f32x4*>(src + 4 * ((2 * cc + 1) ^ (row & 7)));
    }
    u32x4 o = {pack_bf16(s0[0], s0[1]), pack_bf16(s0[2], s0[3]), pack_bf16(s1[0], s1[1]), pack_bf16(s1[2], s1[3])};
    *reinterpret_cast<u32x4*>(out + ((size_t)(b * kHQ + 2 * h) * kSQ + row) * kD + 8 * cc) = o;
  }
}

}

extern "C" void kernel_launch(void* const* d_in, const int* in_sizes, int n_in, void* d_out, int out_size, void* d_ws,
                              size_t ws_size, hipStream_t stream) {
  const unsigned short* q = (const unsigned short*)d_in[0];
  const unsigned short* kc = (const unsigned short*)d_in[1];
  const unsigned short* vc = (const unsigned short*)d_in[2];
  const int* bt = (const int*)d_in[3];
  const int* cl = (const int*)d_in[4];
  const unsigned kv_bytes = (unsigned)in_sizes[1] * 2u;
  (void)hipFuncSetAttribute((const void*)paged_attn_kernel, hipFuncAttributeMaxDynamicSharedMemorySize, kLdsBytes);
  hipLaunchKernelGGL(paged_attn_kernel, dim3(kB * kHKV), dim3(kNW * 64), kLdsBytes, stream, q, kc, vc, bt, cl,
                     (unsigned short*)d_out, kv_bytes);
}
```

```cpp
#include <hip/hip_runtime.h>
#include <hip/hip_bf16.h>
#include <stdint.h>

namespace {

constexpr int kB = 16, kHQ = 32, kHKV = 16, kSQ = 16, kD = 128, kPage = 16, kNBPS = 256, kSKV = kNBPS * kPage;
constexpr int kNW = 8;
constexpr int kTokBytes = kHKV * kD * 2;
constexpr int kPageBytes = kPage * kTokBytes;
constexpr int kAuxNT = 2;
#define PACE_TT 6100
#define MAPX4 0

typedef __attribute__((ext_vector_type(8))) __bf16 bf16x8;
typedef __attribute__((ext_vector_type(2))) __bf16 bf16x2;
typedef __attribute__((ext_vector_type(4))) float f32x4;
typedef __attribute__((ext_vector_type(4))) unsigned u32x4;
typedef __attribute__((ext_vector_type(2))) int i32x2;
#define LAS __attribute__((address_space(3)))

__device__ __forceinline__ f32x4 mfma16(u32x4 a, u32x4 b, f32x4 c) {
  return __builtin_amdgcn_mfma_f32_16x16x32_bf16(__builtin_bit_cast(bf16x8, a), __builtin_bit_cast(bf16x8, b), c, 0, 0, 0);
}
__device__ __forceinline__ unsigned pack_bf16(float lo, float hi) {
  bf16x2 r = {(__bf16)lo, (__bf16)hi};
  return __builtin_bit_cast(unsigned, r);
}
__device__ __forceinline__ float xq_max(float x) {
  auto s = __builtin_amdgcn_permlane16_swap(__float_as_uint(x), __float_as_uint(x), false, false);
  x = fmaxf(__uint_as_float(s[0]), __uint_as_float(s[1]));
  auto t = __builtin_amdgcn_permlane32_swap(__float_as_uint(x), __float_as_uint(x), false, false);
  return fmaxf(__uint_as_float(t[0]), __uint_as_float(t[1]));
}
__device__ __forceinline__ float xq_sum(float x) {
  auto s = __builtin_amdgcn_permlane16_swap(__float_as_uint(x), __float_as_uint(x), false, false);
  x = __uint_as_float(s[0]) + __uint_as_float(s[1]);
  auto t = __builtin_amdgcn_permlane32_swap(__float_as_uint(x), __float_as_uint(x), false, false);
  return __uint_as_float(t[0]) + __uint_as_float(t[1]);
}
__device__ __forceinline__ unsigned now_ticks() { return (unsigned)__builtin_amdgcn_s_memrealtime(); }

constexpr int kWaveLds = 32 * kD * 4;
constexpr int kLdsBytes = kNW * kWaveLds + kNW * 32 * 2 * 4;

__global__ void __launch_bounds__(kNW * 64, 2)
paged_attn_kernel(const unsigned short* __restrict__ q, const unsigned short* __restrict__ kc,
                  const unsigned short* __restrict__ vc, const int* __restrict__ bt,
                  const int* __restrict__ cl, unsigned short* __restrict__ out, unsigned long long* __restrict__ ws,
                  unsigned kv_bytes, unsigned pace_tt) {
  extern __shared__ __attribute__((aligned(16))) unsigned char smem[];
  const unsigned t0 = now_ticks();
  const int b = blockIdx.x >> 4, h = blockIdx.x & 15;
  const int tid = threadIdx.x, lane = tid & 63;
  const int wave = __builtin_amdgcn_readfirstlane(tid >> 6);
  const int g4 = lane >> 4, c16 = lane & 15;

  const i32x2 btA = *reinterpret_cast<const i32x2*>(bt + b * kNBPS + 2 * lane);
  const i32x2 btB = *reinterpret_cast<const i32x2*>(bt + b * kNBPS + 128 + 2 * lane);
  u32x4 Q[2][4];
  {
    const unsigned short* qb = q + ((size_t)(b * kHQ + 2 * h) * kSQ) * kD;
#pragma unroll
    for (int qt = 0; qt < 2; ++qt)
#pragma unroll
      for (int s = 0; s < 4; ++s)
        Q[qt][s] = *reinterpret_cast<const u32x4*>(qb + (qt * kSQ + c16) * kD + 32 * s + 8 * g4);
  }
  int total_tiles = 0;
#pragma unroll
  for (int i = 0; i < kB; ++i) {
    int li = cl[i];
    li = li <= 0 ? kSKV : min(li, kSKV);
    total_tiles += (li + 31) >> 5;
  }
  int len = cl[b];
  float cscale = 0.08838834764831845f * 1.4426950408889634f;
  if (len <= 0) { len = kSKV; cscale = 0.f; }
  len = min(len, kSKV);
  const int npages = (len + kPage - 1) / kPage;
  const int ntiles = (npages + 1) >> 1;
  const unsigned step = (unsigned)((float)pace_tt * (256.f / 1560.f) * (float)total_tiles / (float)ntiles);

  const __amdgpu_buffer_rsrc_t krs = __builtin_amdgcn_make_buffer_rsrc((void*)kc, 0, kv_bytes, 0x00020000);
  const __amdgpu_buffer_rsrc_t vrs = __builtin_amdgcn_make_buffer_rsrc((void*)vc, 0, kv_bytes, 0x00020000);
  int koff[4];
#pragma unroll
  for (int i = 0; i < 4; ++i) koff[i] = (4 * i + g4) * kTokBytes + ((c16 ^ (4 * i + g4)) * 16);
  const int voff = (4 * g4) * kTokBytes + c16 * 16;
  unsigned char* wl = smem + wave * kWaveLds;
  int kfr[4];
#pragma unroll
  for (int s = 0; s < 4; ++s) kfr[s] = c16 * 256 + (((4 * s + g4) ^ c16) * 16);

  f32x4 acc[2][8];
#pragma unroll
  for (int qt = 0; qt < 2; ++qt)
#pragma unroll
    for (int e = 0; e < 8; ++e) acc[qt][e] = f32x4{0.f, 0.f, 0.f, 0.f};
  float m[2] = {-INFINITY, -INFINITY}, l[2] = {0.f, 0.f};

  for (int j = (ntiles - 1 - wave) % kNW; j < ntiles && j >= 0; j += kNW) {
    int p0, p1;
    if (j < 64) { p0 = __builtin_amdgcn_readlane(btA[0], j); p1 = __builtin_amdgcn_readlane(btA[1], j); }
    else { p0 = __builtin_amdgcn_readlane(btB[0], j - 64); p1 = __builtin_amdgcn_readlane(btB[1], j - 64); }
    if (2 * j + 1 >= npages) p1 = p0;
    const int base[2] = {p0 * kPageBytes + h * (kD * 2), p1 * kPageBytes + h * (kD * 2)};
    {
      const unsigned due = t0 + (((unsigned)j * step) >> 8);
      while ((int)(now_ticks() - due) < 0) __builtin_amdgcn_s_sleep(1);
    }
#pragma unroll
    for (int pg = 0; pg < 2; ++pg)
#pragma unroll
      for (int i = 0; i < 4; ++i)
        __builtin_amdgcn_raw_ptr_buffer_load_lds(krs, (LAS void*)(wl + pg * 4096 + i * 1024), 16, koff[i], base[pg], 0, kAuxNT);
    u32x4 V[2][4];
#pragma unroll
    for (int pg = 0; pg < 2; ++pg)
#pragma unroll
      for (int t = 0; t < 4; ++t) V[pg][t] = __builtin_amdgcn_raw_buffer_load_b128(vrs, voff, base[pg] + t * kTokBytes, kAuxNT);
    __builtin_amdgcn_sched_barrier(0);
    asm volatile("s_waitcnt vmcnt(8)" ::: "memory");
    __builtin_amdgcn_sched_barrier(0);

    f32x4 S[2][2];
#pragma unroll
    for (int pg = 0; pg < 2; ++pg) {
      u32x4 K[4];
#pragma unroll
      for (int s = 0; s < 4; ++s) K[s] = *reinterpret_cast<const u32x4*>(wl + pg * 4096 + kfr[s]);
#pragma unroll
      for (int qt = 0; qt < 2; ++qt) {
        f32x4 a = {0.f, 0.f, 0.f, 0.f};
#pragma unroll
        for (int s = 0; s < 4; ++s) a = mfma16(K[s], Q[qt][s], a);
        S[pg][qt] = a;
      }
    }
    const int tok0 = 32 * j;
    if (tok0 + 32 > len) {
#pragma unroll
      for (int pg = 0; pg < 2; ++pg)
#pragma unroll
        for (int r = 0; r < 4; ++r) {
          const bool dead = tok0 + 16 * pg + 4 * g4 + r >= len;
#pragma unroll
          for (int qt = 0; qt < 2; ++qt) S[pg][qt][r] = dead ? -INFINITY : S[pg][qt][r];
        }
    }
    u32x4 P[2];
#pragma unroll
    for (int qt = 0; qt < 2; ++qt) {
      float mx = fmaxf(fmaxf(fmaxf(S[0][qt][0], S[0][qt][1]), fmaxf(S[0][qt][2], S[0][qt][3])),
                       fmaxf(fmaxf(S[1][qt][0], S[1][qt][1]), fmaxf(S[1][qt][2], S[1][qt][3])));
      mx = xq_max(mx);
      const float mn = fmaxf(m[qt], mx * cscale);
      const float alpha = __builtin_amdgcn_exp2f(m[qt] - mn);
      m[qt] = mn;
      float p[2][4];
      float ps = 0.f;
#pragma unroll
      for (int pg = 0; pg < 2; ++pg)
#pragma unroll
        for (int r = 0; r < 4; ++r) {
          p[pg][r] = __builtin_amdgcn_exp2f(fmaf(S[pg][qt][r], cscale, -mn));
          ps += p[pg][r];
        }
      l[qt] = fmaf(l[qt], alpha, ps);
      P[qt] = u32x4{pack_bf16(p[0][0], p[0][1]), pack_bf16(p[0][2], p[0][3]), pack_bf16(p[1][0], p[1][1]), pack_bf16(p[1][2], p[1][3])};
#pragma unroll
      for (int e = 0; e < 8; ++e) acc[qt][e] *= alpha;
    }
#pragma unroll
    for (int e = 0; e < 8; ++e) {
      const unsigned sel = (e & 1) ? 0x07060302u : 0x05040100u;
      u32x4 A;
#pragma unroll
      for (int w = 0; w < 4; ++w)
        A[w] = __builtin_amdgcn_perm(V[w >> 1][2 * (w & 1) + 1][e >> 1], V[w >> 1][2 * (w & 1)][e >> 1], sel);
#pragma unroll
      for (int qt = 0; qt < 2; ++qt) acc[qt][e] = mfma16(A, P[qt], acc[qt][e]);
    }
  }

  float* sM = reinterpret_cast<float*>(smem + kNW * kWaveLds);
  float* sL = sM + kNW * 32;
#pragma unroll
  for (int qt = 0; qt < 2; ++qt) {
    l[qt] = xq_sum(l[qt]);
    if (g4 == 0) { sM[(qt * 16 + c16) * kNW + wave] = m[qt]; sL[(qt * 16 + c16) * kNW + wave] = l[qt]; }
    float* dst = reinterpret_cast<float*>(wl) + (qt * 16 + c16) * kD;
#pragma unroll
    for (int r = 0; r < 4; ++r)
#pragma unroll
      for (int eh = 0; eh < 2; ++eh) {
        f32x4 v = {acc[qt][4 * eh + 0][r], acc[qt][4 * eh + 1][r], acc[qt][4 * eh + 2][r], acc[qt][4 * eh + 3][r]};
        const int chunk = (8 * g4 + 2 * r + eh) ^ (c16 & 7);
        *reinterpret_cast<f32x4*>(dst + 4 * chunk) = v;
      }
  }
  __syncthreads();
  {
    const int row = tid >> 4, cc = tid & 15;
    const f32x4 m0 = *reinterpret_cast<const f32x4*>(sM + row * kNW), m1 = *reinterpret_cast<const f32x4*>(sM + row * kNW + 4);
    const f32x4 l0 = *reinterpret_cast<const f32x4*>(sL + row * kNW), l1 = *reinterpret_cast<const f32x4*>(sL + row * kNW + 4);
    const float mw[kNW] = {m0[0], m0[1], m0[2], m0[3], m1[0], m1[1], m1[2], m1[3]};
    const float lw[kNW] = {l0[0], l0[1], l0[2], l0[3], l1[0], l1[1], l1[2], l1[3]};
    float M = mw[0];
#pragma unroll
    for (int w = 1; w < kNW; ++w) M = fmaxf(M, mw[w]);
    float L = 0.f;
    f32x4 s0 = {0.f, 0.f, 0.f, 0.f}, s1 = {0.f, 0.f, 0.f, 0.f};
#pragma unroll
    for (int w = 0; w < kNW; ++w) {
      const float f = __builtin_amdgcn_exp2f(mw[w] - M);
      L = fmaf(lw[w], f, L);
      const float* src = reinterpret_cast<const float*>(smem + w * kWaveLds) + row * kD;
      s0 += f * *reinterpret_cast<const f32x4*>(src + 4 * ((2 * cc) ^ (row & 7)));
      s1 += f * *reinterpret_cast<const f32x4*>(src + 4 * ((2 * cc + 1) ^ (row & 7)));
    }
    const float inv = 1.f / L;
    s0 *= inv; s1 *= inv;
    u32x4 o = {pack_bf16(s0[0], s0[1]), pack_bf16(s0[2], s0[3]), pack_bf16(s1[0], s1[1]), pack_bf16(s1[2], s1[3])};
    *reinterpret_cast<u32x4*>(out + ((size_t)(b * kHQ + 2 * h) * kSQ + row) * kD + 8 * cc) = o;
  }
}

}

extern "C" void kernel_launch(void* const* d_in, const int* in_sizes, int n_in, void* d_out, int out_size, void* d_ws,
                              size_t ws_size, hipStream_t stream) {
  const unsigned short* q = (const unsigned short*)d_in[0];
  const unsigned short* kc = (const unsigned short*)d_in[1];
  const unsigned short* vc = (const unsigned short*)d_in[2];
  const int* bt = (const int*)d_in[3];
  const int* cl = (const int*)d_in[4];
  const unsigned kv_bytes = (unsigned)in_sizes[1] * 2u;
  const unsigned pace = PACE_TT;
  (void)hipFuncSetAttribute((const void*)paged_attn_kernel, hipFuncAttributeMaxDynamicSharedMemorySize, kLdsBytes);
  hipLaunchKernelGGL(paged_attn_kernel, dim3(kB * kHKV), dim3(kNW * 64), kLdsBytes, stream, q, kc, vc, bt, cl,
                     (unsigned short*)d_out, (unsigned long long*)d_ws, kv_bytes, pace);
}
```

```cpp
#define KAUX 2
#define VAUX 2
#include <hip/hip_runtime.h>
#include <hip/hip_bf16.h>
#include <stdint.h>

namespace {

constexpr int kB = 16, kHQ = 32, kHKV = 16, kSQ = 16, kD = 128, kPage = 16, kNBPS = 256, kSKV = kNBPS * kPage;
constexpr int kNW = 8;
constexpr int kTokBytes = kHKV * kD * 2;
constexpr int kPageBytes = kPage * kTokBytes;
#define KVAUX 2
#define PACE_TT 0
#define MAPX4 0

typedef __attribute__((ext_vector_type(8))) __bf16 bf16x8;
typedef __attribute__((ext_vector_type(2))) __bf16 bf16x2;
typedef __attribute__((ext_vector_type(4))) float f32x4;
typedef __attribute__((ext_vector_type(4))) unsigned u32x4;
typedef __attribute__((ext_vector_type(2))) int i32x2;
#define LAS __attribute__((address_space(3)))

__device__ __forceinline__ f32x4 mfma16(u32x4 a, u32x4 b, f32x4 c) {
  return __builtin_amdgcn_mfma_f32_16x16x32_bf16(__builtin_bit_cast(bf16x8, a), __builtin_bit_cast(bf16x8, b), c, 0, 0, 0);
}
__device__ __forceinline__ unsigned pack_bf16(float lo, float hi) {
  bf16x2 r = {(__bf16)lo, (__bf16)hi};
  return __builtin_bit_cast(unsigned, r);
}
__device__ __forceinline__ float xq_max(float x) {
  auto s = __builtin_amdgcn_permlane16_swap(__float_as_uint(x), __float_as_uint(x), false, false);
  x = fmaxf(__uint_as_float(s[0]), __uint_as_float(s[1]));
  auto t = __builtin_amdgcn_permlane32_swap(__float_as_uint(x), __float_as_uint(x), false, false);
  return fmaxf(__uint_as_float(t[0]), __uint_as_float(t[1]));
}
__device__ __forceinline__ float xq_sum(float x) {
  auto s = __builtin_amdgcn_permlane16_swap(__float_as_uint(x), __float_as_uint(x), false, false);
  x = __uint_as_float(s[0]) + __uint_as_float(s[1]);
  auto t = __builtin_amdgcn_permlane32_swap(__float_as_uint(x), __float_as_uint(x), false, false);
  return __uint_as_float(t[0]) + __uint_as_float(t[1]);
}
__device__ __forceinline__ unsigned now_ticks() { return (unsigned)__builtin_amdgcn_s_memrealtime(); }

constexpr int kWaveLds = 32 * kD * 4;
constexpr int kLdsBytes = kNW * kWaveLds + kNW * 32 * 2 * 4;

__global__ void __launch_bounds__(kNW * 64, 2)
paged_attn_kernel(const unsigned short* __restrict__ q, const unsigned short* __restrict__ kc,
                  const unsigned short* __restrict__ vc, const int* __restrict__ bt,
                  const int* __restrict__ cl, unsigned short* __restrict__ out, unsigned long long* __restrict__ ws,
                  unsigned kv_bytes, unsigned pace_tt) {
  extern __shared__ __attribute__((aligned(16))) unsigned char smem[];
  const unsigned t0 = now_ticks();
  const int b = blockIdx.x >> 4, h = blockIdx.x & 15;
  const int tid = threadIdx.x, lane = tid & 63;
  const int wave = __builtin_amdgcn_readfirstlane(tid >> 6);
  const int g4 = lane >> 4, c16 = lane & 15;

  const i32x2 btA = *reinterpret_cast<const i32x2*>(bt + b * kNBPS + 2 * lane);
  const i32x2 btB = *reinterpret_cast<const i32x2*>(bt + b * kNBPS + 128 + 2 * lane);
  u32x4 Q[2][4];
  {
    const unsigned short* qb = q + ((size_t)(b * kHQ + 2 * h) * kSQ) * kD;
#pragma unroll
    for (int qt = 0; qt < 2; ++qt)
#pragma unroll
      for (int s = 0; s < 4; ++s)
        Q[qt][s] = *reinterpret_cast<const u32x4*>(qb + (qt * kSQ + c16) * kD + 32 * s + 8 * g4);
  }
  int total_tiles = 0;
#pragma unroll
  for (int i = 0; i < kB; ++i) {
    int li = cl[i];
    li = li <= 0 ? kSKV : min(li, kSKV);
    total_tiles += (li + 31) >> 5;
  }
  int len = cl[b];
  float cscale = 0.08838834764831845f * 1.4426950408889634f;
  if (len <= 0) { len = kSKV; cscale = 0.f; }
  len = min(len, kSKV);
  const int npages = (len + kPage - 1) / kPage;
  const int ntiles = (npages + 1) >> 1;
  const unsigned step = (unsigned)((float)pace_tt * (256.f / 1560.f) * (float)total_tiles / (float)ntiles);

  const __amdgpu_buffer_rsrc_t krs = __builtin_amdgcn_make_buffer_rsrc((void*)kc, 0, kv_bytes, 0x00020000);
  const __amdgpu_buffer_rsrc_t vrs = __builtin_amdgcn_make_buffer_rsrc((void*)vc, 0, kv_bytes, 0x00020000);
  int koff[4];
#pragma unroll
  for (int i = 0; i < 4; ++i) koff[i] = (4 * i + g4) * kTokBytes + ((c16 ^ (4 * i + g4)) * 16);
  const int voff = (4 * g4) * kTokBytes + c16 * 16;
  unsigned char* wl = smem + wave * kWaveLds;
  int kfr[4];
#pragma unroll
  for (int s = 0; s < 4; ++s) kfr[s] = c16 * 256 + (((4 * s + g4) ^ c16) * 16);

  f32x4 acc[2][8];
#pragma unroll
  for (int qt = 0; qt < 2; ++qt)
#pragma unroll
    for (int e = 0; e < 8; ++e) acc[qt][e] = f32x4{0.f, 0.f, 0.f, 0.f};
  float m[2] = {-INFINITY, -INFINITY}, l[2] = {0.f, 0.f};

  for (int j = (ntiles - 1 - wave) % kNW; j < ntiles && j >= 0; j += kNW) {
    int p0, p1;
    if (j < 64) { p0 = __builtin_amdgcn_readlane(btA[0], j); p1 = __builtin_amdgcn_readlane(btA[1], j); }
    else { p0 = __builtin_amdgcn_readlane(btB[0], j - 64); p1 = __builtin_amdgcn_readlane(btB[1], j - 64); }
    if (2 * j + 1 >= npages) p1 = p0;
    const int base[2] = {p0 * kPageBytes + h * (kD * 2), p1 * kPageBytes + h * (kD * 2)};
    {
      const unsigned due = t0 + (((unsigned)j * step) >> 8);
      while ((int)(now_ticks() - due) < 0) __builtin_amdgcn_s_sleep(1);
    }
#pragma unroll
    for (int pg = 0; pg < 2; ++pg)
#pragma unroll
      for (int i = 0; i < 4; ++i)
        __builtin_amdgcn_raw_ptr_buffer_load_lds(krs, (LAS void*)(wl + pg * 4096 + i * 1024), 16, koff[i], base[pg], 0, KAUX);
    u32x4 V[2][4];
#pragma unroll
    for (int pg = 0; pg < 2; ++pg)
#pragma unroll
      for (int t = 0; t < 4; ++t) V[pg][t] = __builtin_amdgcn_raw_buffer_load_b128(vrs, voff, base[pg] + t * kTokBytes, VAUX);
    __builtin_amdgcn_sched_barrier(0);
    asm volatile("s_waitcnt vmcnt(8)" ::: "memory");
    __builtin_amdgcn_sched_barrier(0);

    f32x4 S[2][2];
#pragma unroll
    for (int pg = 0; pg < 2; ++pg) {
      u32x4 K[4];
#pragma unroll
      for (int s = 0; s < 4; ++s) K[s] = *reinterpret_cast<const u32x4*>(wl + pg * 4096 + kfr[s]);
#pragma unroll
      for (int qt = 0; qt < 2; ++qt) {
        f32x4 a = {0.f, 0.f, 0.f, 0.f};
#pragma unroll
        for (int s = 0; s < 4; ++s) a = mfma16(K[s], Q[qt][s], a);
        S[pg][qt] = a;
      }
    }
    const int tok0 = 32 * j;
    if (tok0 + 32 > len) {
#pragma unroll
      for (int pg = 0; pg < 2; ++pg)
#pragma unroll
        for (int r = 0; r < 4; ++r) {
          const bool dead = tok0 + 16 * pg + 4 * g4 + r >= len;
#pragma unroll
          for (int qt = 0; qt < 2; ++qt) S[pg][qt][r] = dead ? -INFINITY : S[pg][qt][r];
        }
    }
    u32x4 P[2];
#pragma unroll
    for (int qt = 0; qt < 2; ++qt) {
      float mx = fmaxf(fmaxf(fmaxf(S[0][qt][0], S[0][qt][1]), fmaxf(S[0][qt][2], S[0][qt][3])),
                       fmaxf(fmaxf(S[1][qt][0], S[1][qt][1]), fmaxf(S[1][qt][2], S[1][qt][3])));
      mx = xq_max(mx);
      const float mn = fmaxf(m[qt], mx * cscale);
      const float alpha = __builtin_amdgcn_exp2f(m[qt] - mn);
      m[qt] = mn;
      float p[2][4];
      float ps = 0.f;
#pragma unroll
      for (int pg = 0; pg < 2; ++pg)
#pragma unroll
        for (int r = 0; r < 4; ++r) {
          p[pg][r] = __builtin_amdgcn_exp2f(fmaf(S[pg][qt][r], cscale, -mn));
          ps += p[pg][r];
        }
      l[qt] = fmaf(l[qt], alpha, ps);
      P[qt] = u32x4{pack_bf16(p[0][0], p[0][1]), pack_bf16(p[0][2], p[0][3]), pack_bf16(p[1][0], p[1][1]), pack_bf16(p[1][2], p[1][3])};
#pragma unroll
      for (int e = 0; e < 8; ++e) acc[qt][e] *= alpha;
    }
#pragma unroll
    for (int e = 0; e < 8; ++e) {
      const unsigned sel = (e & 1) ? 0x07060302u : 0x05040100u;
      u32x4 A;
#pragma unroll
      for (int w = 0; w < 4; ++w)
        A[w] = __builtin_amdgcn_perm(V[w >> 1][2 * (w & 1) + 1][e >> 1], V[w >> 1][2 * (w & 1)][e >> 1], sel);
#pragma unroll
      for (int qt = 0; qt < 2; ++qt) acc[qt][e] = mfma16(A, P[qt], acc[qt][e]);
    }
  }

  float* sM = reinterpret_cast<float*>(smem + kNW * kWaveLds);
  float* sL = sM + kNW * 32;
#pragma unroll
  for (int qt = 0; qt < 2; ++qt) {
    l[qt] = xq_sum(l[qt]);
    if (g4 == 0) { sM[(qt * 16 + c16) * kNW + wave] = m[qt]; sL[(qt * 16 + c16) * kNW + wave] = l[qt]; }
    float* dst = reinterpret_cast<float*>(wl) + (qt * 16 + c16) * kD;
#pragma unroll
    for (int r = 0; r < 4; ++r)
#pragma unroll
      for (int eh = 0; eh < 2; ++eh) {
        f32x4 v = {acc[qt][4 * eh + 0][r], acc[qt][4 * eh + 1][r], acc[qt][4 * eh + 2][r], acc[qt][4 * eh + 3][r]};
        const int chunk = (8 * g4 + 2 * r + eh) ^ (c16 & 7);
        *reinterpret_cast<f32x4*>(dst + 4 * chunk) = v;
      }
  }
  __syncthreads();
  {
    const int row = tid >> 4, cc = tid & 15;
    const f32x4 m0 = *reinterpret_cast<const f32x4*>(sM + row * kNW), m1 = *reinterpret_cast<const f32x4*>(sM + row * kNW + 4);
    const f32x4 l0 = *reinterpret_cast<const f32x4*>(sL + row * kNW), l1 = *reinterpret_cast<const f32x4*>(sL + row * kNW + 4);
    const float mw[kNW] = {m0[0], m0[1], m0[2], m0[3], m1[0], m1[1], m1[2], m1[3]};
    const float lw[kNW] = {l0[0], l0[1], l0[2], l0[3], l1[0], l1[1], l1[2], l1[3]};
    float M = mw[0];
#pragma unroll
    for (int w = 1; w < kNW; ++w) M = fmaxf(M, mw[w]);
    float L = 0.f;
    f32x4 s0 = {0.f, 0.f, 0.f, 0.f}, s1 = {0.f, 0.f, 0.f, 0.f};
#pragma unroll
    for (int w = 0; w < kNW; ++w) {
      const float f = __builtin_amdgcn_exp2f(mw[w] - M);
      L = fmaf(lw[w], f, L);
      const float* src = reinterpret_cast<const float*>(smem + w * kWaveLds) + row * kD;
      s0 += f * *reinterpret_cast<const f32x4*>(src + 4 * ((2 * cc) ^ (row & 7)));
      s1 += f * *reinterpret_cast<const f32x4*>(src + 4 * ((2 * cc + 1) ^ (row & 7)));
    }
    const float inv = 1.f / L;
    s0 *= inv; s1 *= inv;
    u32x4 o = {pack_bf16(s0[0], s0[1]), pack_bf16(s0[2], s0[3]), pack_bf16(s1[0], s1[1]), pack_bf16(s1[2], s1[3])};
    *reinterpret_cast<u32x4*>(out + ((size_t)(b * kHQ + 2 * h) * kSQ + row) * kD + 8 * cc) = o;
  }
}

}

extern "C" void kernel_launch(void* const* d_in, const int* in_sizes, int n_in, void* d_out, int out_size, void* d_ws,
                              size_t ws_size, hipStream_t stream) {
  const unsigned short* q = (const unsigned short*)d_in[0];
  const unsigned short* kc = (const unsigned short*)d_in[1];
  const unsigned short* vc = (const unsigned short*)d_in[2];
  const int* bt = (const int*)d_in[3];
  const int* cl = (const int*)d_in[4];
  const unsigned kv_bytes = (unsigned)in_sizes[1] * 2u;
  const unsigned pace = PACE_TT;
  (void)hipFuncSetAttribute((const void*)paged_attn_kernel, hipFuncAttributeMaxDynamicSharedMemorySize, kLdsBytes);
  hipLaunchKernelGGL(paged_attn_kernel, dim3(kB * kHKV), dim3(kNW * 64), kLdsBytes, stream, q, kc, vc, bt, cl,
                     (unsigned short*)d_out, (unsigned long long*)d_ws, kv_bytes, pace);
}
```

```cpp
#include <hip/hip_runtime.h>
#include <hip/hip_bf16.h>
#include <stdint.h>

namespace {

constexpr int kB = 16, kHQ = 32, kHKV = 16, kSQ = 16, kD = 128, kPage = 16, kNBPS = 256, kSKV = kNBPS * kPage;
constexpr int kNW = 8;
constexpr int kTokBytes = kHKV * kD * 2;
constexpr int kPageBytes = kPage * kTokBytes;
constexpr int kQOBytes = kB * kHQ * kSQ * kD * 2;
constexpr int kAuxNT = 2, kAuxSC1 = 16;

constexpr unsigned kPace = 10623;
constexpr int kOrigin = 20;
constexpr int kSlowLead = 6;
constexpr int kCap256 = 280;
constexpr int kLendMax = 19, kLendTail = 3;
constexpr int kEarlyTiles = 18;
constexpr int kPublishLead = 270;
constexpr int kStallHeld = 30;
constexpr int kStallWait = 40;
constexpr int kStallLat = 280, kStallLatSlow = 390;
constexpr int kStallWindow = 150;
constexpr int kStallQuiet = 500;
constexpr int kStallShift = 300;
constexpr int kStallEnd = 500;
constexpr unsigned kStallBusy = 14;
constexpr int kCoarsePts = 24;
constexpr float kLazyThr = 8.f;
constexpr int kWarmWGs = 8;

typedef __attribute__((ext_vector_type(8))) __bf16 bf16x8;
typedef __attribute__((ext_vector_type(2))) __bf16 bf16x2;
typedef __attribute__((ext_vector_type(2))) float f32x2;
typedef __attribute__((ext_vector_type(4))) float f32x4;
typedef __attribute__((ext_vector_type(4))) unsigned u32x4;
typedef __attribute__((ext_vector_type(16))) unsigned u32x16;
typedef __attribute__((ext_vector_type(32))) unsigned u32x32;
typedef __attribute__((ext_vector_type(2))) int i32x2;
typedef __attribute__((ext_vector_type(2))) unsigned u32x2;
typedef unsigned long long u64;
#define LAS __attribute__((address_space(3)))
#define GAS __attribute__((address_space(1)))
typedef LAS unsigned char lds_u8;
typedef __amdgpu_buffer_rsrc_t rsrc_t;

__device__ __forceinline__ rsrc_t make_rsrc(const void* p, unsigned bytes) {
  return __builtin_amdgcn_make_buffer_rsrc((void*)p, 0, bytes, 0x00020000);
}
__device__ __forceinline__ f32x4 mfma16(u32x4 a, u32x4 b, f32x4 c) {
  return __builtin_amdgcn_mfma_f32_16x16x32_bf16(__builtin_bit_cast(bf16x8, a), __builtin_bit_cast(bf16x8, b), c, 0, 0, 0);
}
__device__ __forceinline__ unsigned pack_bf16(float lo, float hi) {
  bf16x2 r = {(__bf16)lo, (__bf16)hi};
  return __builtin_bit_cast(unsigned, r);
}
__device__ __forceinline__ float bf_lo(unsigned x) { return __uint_as_float(x << 16); }
__device__ __forceinline__ float bf_hi(unsigned x) { return __uint_as_float(x & 0xffff0000u); }
__device__ __forceinline__ float max3(float a, float b, float c) {
  float r;
  asm("v_max3_f32 %0, %1, %2, %3" : "=v"(r) : "v"(a), "v"(b), "v"(c));
  return r;
}
__device__ __forceinline__ float xq_max(float x) {
  auto s = __builtin_amdgcn_permlane16_swap(__float_as_uint(x), __float_as_uint(x), false, false);
  x = fmaxf(__uint_as_float(s[0]), __uint_as_float(s[1]));
  auto t = __builtin_amdgcn_permlane32_swap(__float_as_uint(x), __float_as_uint(x), false, false);
  return fmaxf(__uint_as_float(t[0]), __uint_as_float(t[1]));
}
__device__ __forceinline__ unsigned now_ticks() { return (unsigned)__builtin_amdgcn_s_memrealtime(); }
__device__ __forceinline__ int rfl(int x) { return __builtin_amdgcn_readfirstlane(x); }
__device__ __forceinline__ u64 rfl64(u64 x) { return ((u64)(unsigned)rfl((int)(x >> 32)) << 32) | (unsigned)rfl((int)x); }
template <class T> __device__ __forceinline__ T lds_ld(lds_u8* p) { return *reinterpret_cast<LAS T*>(p); }
template <class T> __device__ __forceinline__ void lds_st(lds_u8* p, T v) { *reinterpret_cast<LAS T*>(p) = v; }
__device__ __forceinline__ unsigned lds_addr(lds_u8* p) { return (unsigned)(uintptr_t)p; }
__device__ __forceinline__ unsigned lds_poll32(lds_u8* p) {
  unsigned v;
  asm volatile("ds_read_b32 %0, %1\n\ts_waitcnt lgkmcnt(0)" : "=v"(v) : "v"(lds_addr(p)) : "memory");
  return (unsigned)rfl((int)v);
}
#define WAIT_VM(n) do { __builtin_amdgcn_sched_barrier(0); asm volatile("s_waitcnt vmcnt(" #n ")" ::: "memory"); __builtin_amdgcn_sched_barrier(0); } while (0)

constexpr int kWaveLds = 32 * kD * 4;
constexpr int kLdsQ = kNW * kWaveLds;
constexpr int kQBytes = 8192 + 1024;
constexpr int kLdsML = kLdsQ + 2 * kQBytes;
constexpr int kLdsJunk = kLdsML + 32 * kNW * 8;
constexpr int kLdsCnt = kLdsJunk + 256;
constexpr int kLdsTime = kLdsCnt + 32;
constexpr int kLdsTab = kLdsTime + 32;
constexpr int kLdsPX = kLdsTab + kNW * 256 + 256;
constexpr int kLdsBytes = kLdsPX + 4 * 2048;

constexpr int kSlots = kB * kHKV;
constexpr int kPayBytes = 9 * 1024;
constexpr size_t kWsCtl = 65536, kWsPay = kWsCtl + (size_t)kSlots * 256, kWsEnd = kWsPay + (size_t)kSlots * kPayBytes;
constexpr unsigned kMaxPolls = 1u << 20;

__global__ void __launch_bounds__(kNW * 64, 2)
paged_attn_kernel(const void* q_, const void* kc_, const void* vc_, const void* bt_, const void* cl_, void* out_, void* ws_,
                  unsigned kv_bytes_, unsigned pace_) {
  extern __shared__ __attribute__((aligned(256))) unsigned char smem_[];
  lds_u8* const smem = (lds_u8*)smem_;
  u32x16 ka;
  asm volatile("s_load_dwordx16 %0, %1, 0x0" : "=s"(ka) : "s"(__builtin_amdgcn_kernarg_segment_ptr()));
  const int tid = threadIdx.x, lane = tid & 63;
  const int wave = rfl(tid >> 6);
  const int g4 = lane >> 4, c16 = lane & 15;
  const int bo = (blockIdx.x & 7) + 8 * (blockIdx.x >> 7), ho = (blockIdx.x >> 3) & 15;
  const bool warmer = blockIdx.x < kWarmWGs;
  if (warmer && wave == 3) {
    u64 pc;
    unsigned nbytes;
    asm volatile("s_getpc_b64 %0\n.Lpaged_attn_pc:\n\ts_mov_b32 %1, .Lpaged_attn_end-.Lpaged_attn_pc" : "=s"(pc), "=s"(nbytes));
    const rsrc_t xrs = make_rsrc((void*)pc, nbytes & ~3u);
    __builtin_amdgcn_raw_ptr_buffer_load_lds(xrs, (LAS void*)(smem + kLdsJunk), 4, lane * 128, 0, 0, 0);
    __builtin_amdgcn_raw_ptr_buffer_load_lds(xrs, (LAS void*)(smem + kLdsJunk), 4, lane * 128, 8192, 0, 0);
  }
  lds_u8* const wl = smem + wave * kWaveLds;
  int go[4];
#pragma unroll
  for (int i = 0; i < 4; ++i) go[i] = (4 * i + g4) * kTokBytes + (c16 ^ (4 * i + g4)) * 16;
  unsigned tslot = lds_addr(smem + kLdsTime + 4 * wave);
  int o_bt = (bo * kNBPS + 2 * wave) * 4, o_lo = bo * 4, o_lp = (bo ^ 8) * 4;
  asm volatile("ds_write_b32 %5, %6" : "+v"(go[0]), "+v"(go[1]), "+v"(go[2]), "+v"(go[3]), "+v"(tslot)
               : "v"(lds_addr(smem + kLdsCnt + 4 * (lane & 7))), "v"(0u) : "memory");
  asm volatile("s_waitcnt lgkmcnt(0)" : "+s"(ka), "+s"(o_bt), "+s"(o_lo), "+s"(o_lp) :: "memory");
  auto arg_ptr = [&](int i) { return (unsigned char*)(((u64)ka[2 * i + 1] << 32) | ka[2 * i]); };
  unsigned char *const q = arg_ptr(0), *const kc = arg_ptr(1), *const vc = arg_ptr(2), *const bt = arg_ptr(3), *const cl = arg_ptr(4);
  unsigned char *const out = arg_ptr(5), *const ws = arg_ptr(6);
  const unsigned kv_bytes = ka[14], pace = ka[15];

  i32x2 pfirst;
  int leno, lenp;
  u32x16 lens;
  asm volatile("s_load_dwordx2 %0, %4, %5\n\ts_load_dword %1, %6, %7\n\ts_load_dword %2, %6, %8\n\ts_load_dwordx16 %3, %6, 0x0"
               : "=&s"(pfirst), "=&s"(leno), "=&s"(lenp), "=&s"(lens)
               : "s"(bt), "s"(o_bt), "s"(cl), "s"(o_lo), "s"(o_lp));
  const rsrc_t krs = make_rsrc(kc, kv_bytes), vrs = make_rsrc(vc, kv_bytes);
  const rsrc_t qrs = make_rsrc(q, kQOBytes), brs = make_rsrc(bt, kB * kNBPS * 4);
  if (wave >= 4) {
#pragma unroll
    for (int qt = 0; qt < 2; ++qt)
      __builtin_amdgcn_raw_ptr_buffer_load_lds(qrs, (LAS void*)(smem + kLdsQ + qt * 4096 + (wave & 3) * 1024), 16,
                                               g4 * 256 + ((c16 ^ (4 * (wave & 3) + g4)) * 16),
                                               (bo * kHQ + 2 * ho) * kSQ * kD * 2 + qt * 4096 + (wave & 3) * 1024, 0, 0);
    __builtin_amdgcn_raw_ptr_buffer_load_lds(brs, (LAS void*)(smem + kLdsQ + 8192 + (wave & 3) * 256), 4, lane * 4,
                                             bo * (kNBPS * 4) + (wave & 3) * 256, 0, 0);
  } else if (warmer && wave == 1) {
    __builtin_amdgcn_raw_ptr_buffer_load_lds(krs, (LAS void*)(smem + kLdsJunk), 4,
                                             (int)((kv_bytes / (2 * kCoarsePts)) & ~127u) * (2 * min(lane, kCoarsePts) + 1), 0, 0, 0);
  }
  float tscale = (float)pace * 128e-5f;
  const int cls = (int)((((unsigned)(uintptr_t)kc >> 8) + ho) & 3);
  asm volatile("" : "+v"(tscale));
  asm volatile("s_waitcnt lgkmcnt(0)" : "+s"(pfirst), "+s"(leno), "+s"(lenp), "+s"(lens) :: "memory");
  *(LAS unsigned*)(uintptr_t)tslot = now_ticks();
  auto issue_page = [&](rsrc_t rs, lds_u8* dst, int base) {
#pragma unroll
    for (int i = 0; i < 4; ++i)
      __builtin_amdgcn_raw_ptr_buffer_load_lds(rs, (LAS void*)(dst + i * 1024), 16, go[i], base, 0, kAuxNT);
  };
  if (wave == 0) {
    issue_page(krs, wl, pfirst[0] * kPageBytes + ho * (kD * 2));
    issue_page(krs, wl + 4096, pfirst[1] * kPageBytes + ho * (kD * 2));
  }

  unsigned usum = 0;
#pragma unroll
  for (int i = 0; i < kB; ++i) usum += lens[i];
  int total = max((int)usum, kB);
  asm volatile("" : "+s"(total));
  total = min(total, kB * kSKV);
  tscale *= (float)total;
  int b = bo, h = ho;
  int len = (int)min((unsigned)leno - 1u, (unsigned)kSKV - 1u) + 1;
  int ntiles = (len + 31) >> 5;
  unsigned pstep = (unsigned)rfl((int)(tscale * 16.f * __builtin_amdgcn_rcpf((float)max(len + kPage / 2, kPage))));
  unsigned tb = (cls == 1 ? 0u : kSlowLead * 256u);
  asm volatile("s_waitcnt lgkmcnt(0)" :: "v"(pstep) : "memory");
  __builtin_amdgcn_s_barrier();
  asm volatile("" ::: "memory");
  const unsigned t0 = (unsigned)rfl(lds_ld<int>(smem + kLdsTime)) + (kOrigin - kSlowLead);
  const unsigned thalf = (unsigned)rfl((int)tscale);
  const int tstop = (int)(thalf >> 7) - kStallEnd;

  unsigned tiss = 0, tprev = 0;
  auto issue_tile = [&](const unsigned char* src, lds_u8* dst, int j, int jd, int p0, int p1) {
    unsigned due = tb + (unsigned)(2 * jd) * pstep, ts;
    for (;;) {
      ts = t0 + lds_poll32(smem + kLdsCnt + 16);
      if ((int)(now_ticks() - (ts + (due >> 8))) >= -14) break;
      __builtin_amdgcn_s_sleep(1);
    }
    tprev = tiss;
#pragma unroll
    for (int p = 0; p < 2; ++p) {
      const int rows = len - 32 * j - 16 * p;
      const unsigned pb = (unsigned)(p ? p1 : p0) * kPageBytes;
      const unsigned lim = rows >= kPage ? kv_bytes : (rows > 0 ? min(pb + (unsigned)rows * kTokBytes, kv_bytes) : 0u);
      unsigned tn;
      do tn = now_ticks(); while ((int)(tn - (ts + (due >> 8))) < 0);
      if (p == 0) tiss = tn - t0;
      issue_page(make_rsrc(src, lim), dst + p * 4096, (int)pb + h * (kD * 2));
      due += rows > kPage ? pstep : 0u;
    }
  };
  auto stall_check = [&]() {
    const unsigned tn = now_ticks() - t0;
    const int wt = (int)(tn - tiss - (pstep >> 8));
    if (__builtin_expect(wt > kStallHeld && b == bo, 0)) {
      u32x4 cur;
      asm volatile("ds_read_b128 %0, %1\n\ts_waitcnt lgkmcnt(0)" : "=v"(cur) : "v"(lds_addr(smem + kLdsCnt + 16)) : "memory");
      const bool fresh = (int)(tn - (unsigned)rfl((int)cur[3])) > kStallQuiet;
      const unsigned ep = fresh ? tn : (unsigned)rfl((int)cur[2]);
      asm volatile("ds_write_b64 %0, %1" :: "v"(lds_addr(smem + kLdsCnt + 24)), "v"(u32x2{ep, tn}) : "memory");
      if (fresh) asm volatile("ds_write_b32 %0, %1" :: "v"(lds_addr(smem + kLdsCnt + 4)), "v"(0u) : "memory");
      if (wt > kStallWait && (int)(tn - ep) < kStallWindow && (int)(tn - tprev) > (cls == 1 ? kStallLatSlow : kStallLat)) {
        unsigned seen;
        asm volatile("ds_or_rtn_b32 %0, %1, %2\n\ts_waitcnt lgkmcnt(0)" : "=v"(seen) : "v"(lds_addr(smem + kLdsCnt + 4)), "v"(1u << wave) : "memory");
        const int need = (cls != 1 && pstep < kStallBusy * 256u) ? 2 : 1;
        if (__builtin_popcount((unsigned)rfl((int)seen) | 1u << wave) >= need &&
            (int)(tprev - (unsigned)rfl((int)cur[1])) >= 0 && (int)(tn - (unsigned)rfl((int)cur[0])) < tstop)
          asm volatile("ds_cmpst_b64 %0, %1, %2" :: "v"(lds_addr(smem + kLdsCnt + 16)), "v"(u32x2{cur[0], cur[1]}),
                       "v"(u32x2{cur[0] + kStallShift, tn + kStallShift}) : "memory");
      }
    }
  };

  if (wave >= 4) {
    WAIT_VM(0);
    if (lane == 0) __hip_atomic_fetch_add((LAS unsigned*)(smem + kLdsCnt), 1u, __ATOMIC_RELAXED, __HIP_MEMORY_SCOPE_WORKGROUP);
  }
  if (wave > 0 && wave < min(ntiles, kNW - 1)) issue_tile(kc, wl, wave, wave, pfirst[0], pfirst[1]);

  const int np = (int)(min((unsigned)lenp - 1u, (unsigned)kSKV - 1u) >> 5) + 1;
  const int nlong = max(ntiles, np), nshort = min(ntiles, np);
  int x = min(nlong - ((total * kCap256) >> 17), kLendMax);
  if (x < 3 || nshort > 7 * 16 || nshort + x > nlong || nlong - 10 < 3 * x) x = 0;
  const bool lender = cls == 1 && ntiles > np && x > 0;
  const bool host = cls == 0 && ntiles < np && x > 0;
  const bool helper = host && wave == kNW - 1;
  if (helper) {
    b = bo ^ 8; h = (ho + 1) & 15;
    len = (int)min((unsigned)lenp - 1u, (unsigned)kSKV - 1u) + 1;
    ntiles = np;
    pstep = (unsigned)rfl((int)(tscale * 16.f * __builtin_amdgcn_rcpf((float)max(len + kPage / 2, kPage))));
    tb = 0;
  }
  const float cscale = (helper ? lenp : leno) <= 0 ? 0.f : 0.08838834764831845f * 1.4426950408889634f;
  while (lds_poll32(smem + kLdsCnt) != 4) {}
  asm volatile("" ::: "memory");
  lds_u8* const ql = smem + kLdsQ + (helper ? kQBytes : 0);
  if (helper) {
    __builtin_amdgcn_raw_ptr_buffer_load_lds(brs, (LAS void*)(ql + 8192), 16, lane * 16, b * (kNBPS * 4), 0, 0);
#pragma unroll
    for (int u = 0; u < 8; ++u)
      __builtin_amdgcn_raw_ptr_buffer_load_lds(qrs, (LAS void*)(ql + u * 1024), 16, g4 * 256 + ((c16 ^ (4 * (u & 3) + g4)) * 16),
                                               (b * kHQ + 2 * h) * kSQ * kD * 2 + u * 1024, 0, 0);
    WAIT_VM(0);
  }
  const int nkeep = (lender || helper) ? ntiles - x : ntiles;
  lds_u8* const tab = smem + kLdsTab + wave * 256;
  int cnt, iv0 = 0;
  {
    const int l32 = lane & 31;
    int jt = wave + l32 * (host ? kNW - 1 : kNW);
    bool valid = jt < nkeep;
    if (lender || helper) {
      const int xi = x - min(kLendTail, x - 2);
      const int ginv = (int)((float)(xi << 16) * __builtin_amdgcn_rcpf((float)(nkeep - 8))) + 1;
      if (lender) {
        if (jt >= 8) jt += min((((jt - 8) * ginv) >> 16) + 1, xi);
      } else {
        int qd = (int)((float)(l32 << 16) * __builtin_amdgcn_rcpf((float)ginv));
        qd += qd * ginv < (l32 << 16);
        qd -= (qd - 1) * ginv >= (l32 << 16);
        jt = (l32 < xi ? 8 + qd : nkeep) + l32;
        valid = l32 < x;
      }
    }
    cnt = __builtin_popcount((unsigned)__ballot(valid));
    if (helper) iv0 = __builtin_popcount((unsigned)__ballot(valid && jt < ntiles - kEarlyTiles));
    if (iv0 == cnt) iv0 = 0;
    const i32x2 pp = lds_ld<i32x2>(ql + 8192 + (jt & 127) * 8);
    if (valid) lds_st<u32x4>(tab + l32 * 16, u32x4{(unsigned)pp[0], (unsigned)pp[1], (unsigned)jt, 0u});
    if (wave == kNW - 1 && cnt > 0) issue_tile(kc, wl, rfl(jt), rfl(jt), rfl(pp[0]), rfl(pp[1]));
  }
  int buf = 0;

  float m[2] = {-1e30f, -1e30f};
  f32x4 lsum[2] = {{0.f, 0.f, 0.f, 0.f}, {0.f, 0.f, 0.f, 0.f}};
  u32x32 PS[4];
  const int ex2 = 2 * max(cnt - 16, 0);
  lds_u8* const px = smem + kLdsPX + lane * 16;
  if (cnt > 0) {
    int kfr[4];
#pragma unroll
    for (int s = 0; s < 4; ++s) kfr[s] = c16 * 256 + (((4 * s + g4) ^ c16) * 16);
    u32x4 Q[2][4];
#pragma unroll
    for (int qt = 0; qt < 2; ++qt)
#pragma unroll
      for (int s = 0; s < 4; ++s) Q[qt][s] = lds_ld<u32x4>(ql + qt * 4096 + kfr[s]);

    int jn = helper ? rfl(lds_ld<int>(tab + 8)) : wave;
    for (int i2 = 0, ip = 0;; i2 += 2) {
      const int j = jn;
      const bool kdone = i2 + 2 >= 2 * cnt;
      const u32x4 te = lds_ld<u32x4>(tab + (kdone ? iv0 * 16 : (i2 + 2) * 8));
      jn = rfl((int)te[2]);
      if (kdone) {
#pragma unroll
        for (int i = 0; i < 4; ++i) go[i] = (4 * i + g4) * kTokBytes + ((c16 ^ (2 * ((4 * i + g4) & 7))) * 16);
        tb += thalf;
      }
      if (warmer) {
        if (i2 == (wave < 2 ? 2 : (wave < 4 ? 12 : 6)) && wave < 7) {
          const rsrc_t trs = wave < 5 ? (wave < 2 ? krs : vrs) : make_rsrc(wave == 5 ? out : ws, wave == 5 ? (unsigned)kQOBytes : (unsigned)kWsEnd);
          const int o = wave < 4 ? (int)((kv_bytes >> 7) & ~127u) * (2 * lane + (wave & 1))
                                 : (wave == 4 ? (int)((kv_bytes / (2 * kCoarsePts)) & ~127u) * (2 * min(lane, kCoarsePts) + 1) : lane << 20);
          __builtin_amdgcn_raw_ptr_buffer_load_lds(trs, (LAS void*)(smem + kLdsJunk), 4, o, 0, 0, 0);
        }
      }
      issue_tile(kdone ? vc : kc, wl + (buf ^ 8192), jn, (kdone && iv0) ? 0 : jn, rfl((int)te[0]), rfl((int)te[1]));
      WAIT_VM(8);
      if (!warmer) stall_check();
      f32x4 S[2][2];
#pragma unroll
      for (int pg = 0; pg < 2; ++pg) {
        u32x4 K[4];
#pragma unroll
        for (int s = 0; s < 4; ++s) K[s] = lds_ld<u32x4>(wl + buf + pg * 4096 + kfr[s]);
#pragma unroll
        for (int qt = 0; qt < 2; ++qt) S[pg][qt] = f32x4{0.f, 0.f, 0.f, 0.f};
#pragma unroll
        for (int s = 0; s < 4; ++s)
#pragma unroll
          for (int qt = 0; qt < 2; ++qt) S[pg][qt] = mfma16(K[s], Q[qt][s], S[pg][qt]);
      }
      const int tok0 = 32 * j;
      if (tok0 + 32 > len) {
#pragma unroll
        for (int pg = 0; pg < 2; ++pg)
#pragma unroll
          for (int rr = 0; rr < 4; ++rr) {
            const bool dead = tok0 + 16 * pg + 4 * g4 + rr >= len;
#pragma unroll
            for (int qt = 0; qt < 2; ++qt) S[pg][qt][rr] = dead ? -INFINITY : S[pg][qt][rr];
          }
      }
      float mx[2];
#pragma unroll
      for (int qt = 0; qt < 2; ++qt)
        mx[qt] = fmaxf(fmaxf(fmaxf(S[0][qt][0], S[0][qt][1]), fmaxf(S[0][qt][2], S[0][qt][3])),
                       fmaxf(fmaxf(S[1][qt][0], S[1][qt][1]), fmaxf(S[1][qt][2], S[1][qt][3]))) * cscale;
      if (__any(!(mx[0] - m[0] <= kLazyThr && mx[1] - m[1] <= kLazyThr))) {
        float alpha[2];
#pragma unroll
        for (int qt = 0; qt < 2; ++qt) {
          const float mn = fmaxf(m[qt], __builtin_ceilf(xq_max(mx[qt])));
          alpha[qt] = __builtin_amdgcn_exp2f(m[qt] - mn);
          m[qt] = mn;
          lsum[qt] *= alpha[qt];
        }
#pragma unroll 1
        for (int k = 0; k < min(i2, ex2); ++k) {
          const float al = (k & 1) ? alpha[1] : alpha[0];
          u32x4 v = lds_ld<u32x4>(px + k * 1024);
#pragma unroll
          for (int w = 0; w < 4; ++w) v[w] = pack_bf16(bf_lo(v[w]) * al, bf_hi(v[w]) * al);
          lds_st<u32x4>(px + k * 1024, v);
        }
#pragma unroll 1
        for (int k = 0; k < ip; ++k) {
          const float al = (k & 1) ? alpha[1] : alpha[0];
#pragma unroll
          for (int w = 0; w < 4; ++w) {
            const unsigned v = PS[w][k];
            PS[w][k] = pack_bf16(bf_lo(v) * al, bf_hi(v) * al);
          }
        }
      }
#pragma unroll
      for (int qt = 0; qt < 2; ++qt) {
        float p[2][4];
#pragma unroll
        for (int pg = 0; pg < 2; ++pg)
#pragma unroll
          for (int rr = 0; rr < 4; ++rr) p[pg][rr] = __builtin_amdgcn_exp2f(fmaf(S[pg][qt][rr], cscale, -m[qt]));
        u32x4 P;
#pragma unroll
        for (int w = 0; w < 4; ++w) { P[w] = pack_bf16(p[w >> 1][2 * (w & 1)], p[w >> 1][2 * (w & 1) + 1]); PS[w][ip + qt] = P[w]; }
        lds_st<u32x4>(px + ((i2 < ex2 ? i2 : 6) + qt) * 1024, P);
        lsum[qt] = mfma16(u32x4{0x3f803f80u, 0x3f803f80u, 0x3f803f80u, 0x3f803f80u}, P, lsum[qt]);
      }
      buf ^= 8192;
      ip += i2 < ex2 ? 0 : 2;
      if (kdone) break;
    }
  }
  if (g4 == 0) {
#pragma unroll
    for (int qt = 0; qt < 2; ++qt)
      lds_st<f32x2>(smem + kLdsML + ((qt * 16 + c16) * kNW + wave) * 8, helper ? f32x2{-1e30f, 0.f} : f32x2{m[qt], lsum[qt][0]});
    if (helper) lds_st<f32x4>(ql + c16 * 16, f32x4{m[0], lsum[0][0], m[1], lsum[1][0]});
  }
  asm volatile("ds_or_b32 %0, %1" :: "v"(lds_addr(smem + kLdsCnt + 8)), "v"(1u << wave) : "memory");

  const int vrow = 4 * g4 + (c16 >> 2);
  const int vfr = vrow * 256 + (vrow & 7) * 32 + (c16 & 3) * 8;
  f32x4 acc[2][8];
#pragma unroll
  for (int qt = 0; qt < 2; ++qt)
#pragma unroll
    for (int e = 0; e < 8; ++e) acc[qt][e] = f32x4{0.f, 0.f, 0.f, 0.f};
  unsigned pa0, pa1;
  auto park = [&](int e) {
#pragma unroll
    for (int qt = 0; qt < 2; ++qt) *(LAS f32x4*)(uintptr_t)(((e & 1) ? pa1 : pa0) + (e >> 1) * 128 + qt * 8192) = acc[qt][e];
  };
  auto pv_tile = [&](const u32x4 (&P)[2], const unsigned (&va)[8], bool parking) {
    u32x2 a0[8], a1[8];
    asm volatile(
        "ds_read_b64_tr_b16 %0, %16\n\tds_read_b64_tr_b16 %8, %16 offset:4096\n\t"
        "ds_read_b64_tr_b16 %1, %17\n\tds_read_b64_tr_b16 %9, %17 offset:4096\n\t"
        "ds_read_b64_tr_b16 %2, %18\n\tds_read_b64_tr_b16 %10, %18 offset:4096\n\t"
        "ds_read_b64_tr_b16 %3, %19\n\tds_read_b64_tr_b16 %11, %19 offset:4096\n\t"
        "ds_read_b64_tr_b16 %4, %20\n\tds_read_b64_tr_b16 %12, %20 offset:4096\n\t"
        "ds_read_b64_tr_b16 %5, %21\n\tds_read_b64_tr_b16 %13, %21 offset:4096\n\t"
        "ds_read_b64_tr_b16 %6, %22\n\tds_read_b64_tr_b16 %14, %22 offset:4096\n\t"
        "ds_read_b64_tr_b16 %7, %23\n\tds_read_b64_tr_b16 %15, %23 offset:4096\n\t"
        "s_waitcnt lgkmcnt(0)"
        : "=&v"(a0[0]), "=&v"(a0[1]), "=&v"(a0[2]), "=&v"(a0[3]), "=&v"(a0[4]), "=&v"(a0[5]), "=&v"(a0[6]), "=&v"(a0[7]),
          "=&v"(a1[0]), "=&v"(a1[1]), "=&v"(a1[2]), "=&v"(a1[3]), "=&v"(a1[4]), "=&v"(a1[5]), "=&v"(a1[6]), "=&v"(a1[7])
        : "v"(va[0]), "v"(va[1]), "v"(va[2]), "v"(va[3]), "v"(va[4]), "v"(va[5]), "v"(va[6]), "v"(va[7])
        : "memory");
#pragma unroll
    for (int e = 0; e < 8; ++e) {
#pragma unroll
      for (int qt = 0; qt < 2; ++qt) acc[qt][e] = mfma16(u32x4{a0[e][0], a0[e][1], a1[e][0], a1[e][1]}, P[qt], acc[qt][e]);
      if (parking) {
        if (e >= 2) park(e - 2);
        __builtin_amdgcn_sched_barrier(0);
      }
    }
    if (parking) { park(6); park(7); }
  };
  auto fetch_p = [&](int i2, u32x4 (&P)[2]) {
#pragma unroll
    for (int qt = 0; qt < 2; ++qt)
#pragma unroll
      for (int w = 0; w < 4; ++w) P[qt][w] = PS[w][max(i2 - ex2, 0) + qt];
    if (__builtin_expect(i2 < ex2, 0)) { P[0] = lds_ld<u32x4>(px + i2 * 1024); P[1] = lds_ld<u32x4>(px + i2 * 1024 + 1024); }
  };
  int in = iv0;
  for (int left = cnt - 1; left > 1; --left) {
    const int i2 = 2 * in;
    if (++in == cnt) in = 0;
    const u32x4 te = lds_ld<u32x4>(tab + in * 16);
    const int jn = rfl((int)te[2]);
    issue_tile(vc, wl + (buf ^ 8192), jn, (iv0 && in > iv0) ? 0 : jn, rfl((int)te[0]), rfl((int)te[1]));
    WAIT_VM(8);
    if (left != cnt - 1) stall_check();
    u32x4 P[2];
    unsigned va[8];
    fetch_p(i2, P);
#pragma unroll
    for (int e = 0; e < 8; ++e) va[e] = (lds_addr(wl + buf) + (unsigned)vfr) ^ (unsigned)(e * 32);
    pv_tile(P, va, false);
    buf ^= 8192;
  }

  const bool taker = lender && wave == (nkeep & 7);
  const int slot = b * kHKV + h;
  u64 wEa = (u64)(ws + kWsCtl + (size_t)slot * 256);
  u64 E = 0;
  const int row = tid >> 4;
  float fw[kNW];
  f32x4 ml[4];
  unsigned ma = lds_addr(smem + kLdsML) + row * 64;
  auto load_ml = [&]() {
#pragma unroll
    for (int u = 0; u < 4; ++u) ml[u] = *(LAS f32x4*)(uintptr_t)(ma + 16 * u);
  };
  auto weights = [&]() {
    const float Mx = max3(max3(ml[0][0], ml[0][2], ml[1][0]), max3(ml[1][2], ml[2][0], ml[2][2]), max3(ml[3][0], ml[3][2], ml[3][2]));
    float L = 0.f;
#pragma unroll
    for (int w = 0; w < kNW; ++w) {
      fw[w] = __builtin_amdgcn_exp2f(ml[w >> 1][2 * (w & 1)] - Mx);
      L = fmaf(ml[w >> 1][2 * (w & 1) + 1], fw[w], L);
    }
    const float inv = __builtin_amdgcn_rcpf(L);
#pragma unroll
    for (int w = 0; w < kNW; ++w) fw[w] *= inv;
  };
  pa0 = lds_addr(wl) + c16 * 512 + 16 * (g4 ^ (c16 & 7)); pa1 = pa0 ^ 64;
  unsigned xa0 = lds_addr(smem) + row * 512 + 16 * ((2 * c16) ^ (row & 7)), xa1 = xa0 ^ 16;
  unsigned xb0 = xa0 + 4 * kWaveLds, xb1 = xa1 + 4 * kWaveLds, oo = tid * 16;
  u64 optr = (u64)(out + (bo * kHQ + 2 * ho) * kSQ * kD * 2);
  int plain = (helper || taker) ? 0 : cnt;
  u32x4 P[2];
  unsigned va[8];
  {
    u32x4 Pb[2];
    const int ib = in;
    if (++in >= cnt) in = 0;
    fetch_p(2 * ib, Pb);
    fetch_p(2 * in, P);
#pragma unroll
    for (int e = 0; e < 8; ++e) va[e] = (lds_addr(wl + buf) + (unsigned)vfr) ^ (unsigned)(e * 32);
    while (lds_poll32(smem + kLdsCnt + 8) != 0xffu) __builtin_amdgcn_s_sleep(2);
    load_ml();
    weights();
    asm volatile("" : "+v"(P[0]), "+v"(P[1]), "+v"(Pb[0]), "+v"(Pb[1]),
                      "+v"(va[0]), "+v"(va[1]), "+v"(va[2]), "+v"(va[3]), "+v"(va[4]), "+v"(va[5]), "+v"(va[6]), "+v"(va[7]));
    asm volatile("" : "+v"(fw[0]), "+v"(fw[1]), "+v"(fw[2]), "+v"(fw[3]), "+v"(fw[4]), "+v"(fw[5]), "+v"(fw[6]), "+v"(fw[7]),
                      "+v"(pa0), "+v"(pa1), "+v"(xa0), "+v"(xa1), "+v"(xb0), "+v"(xb1), "+v"(oo), "+v"(ma));
    asm volatile("" : "+s"(optr), "+s"(plain), "+s"(wEa));
    if (cnt > 1) {
      const u32x4 te = lds_ld<u32x4>(tab + in * 16);
      const int jn = rfl((int)te[2]);
      issue_tile(vc, wl + (buf ^ 8192), jn, (iv0 && in > iv0) ? 0 : jn, rfl((int)te[0]), rfl((int)te[1]));
      WAIT_VM(8);
      pv_tile(Pb, va, false);
#pragma unroll
      for (int e = 0; e < 8; ++e) va[e] ^= 8192;
      asm volatile("" : "+v"(va[0]), "+v"(va[1]), "+v"(va[2]), "+v"(va[3]), "+v"(va[4]), "+v"(va[5]), "+v"(va[6]), "+v"(va[7]));
    }
  }
  GAS u64* const wE = (GAS u64*)wEa;
  if (__builtin_expect(plain != 0, 1)) {
    WAIT_VM(0);
    pv_tile(P, va, true);
  } else {
    if (cnt > 0) {
      WAIT_VM(0);
      pv_tile(P, va, false);
      GAS u64* wX = wE + 16;
      const rsrc_t prs = make_rsrc(ws + kWsPay + (size_t)slot * kPayBytes, kPayBytes);
      lds_u8* mw = smem + kLdsML + (c16 * kNW + wave) * 8;
      const u64 Ev = __hip_atomic_load(wE, __ATOMIC_RELAXED, __HIP_MEMORY_SCOPE_AGENT);
      if (helper) {
        while ((int)(now_ticks() - (t0 + lds_poll32(smem + kLdsCnt + 16) + (thalf >> 7) - kPublishLead)) < 0) __builtin_amdgcn_s_sleep(1);
#pragma unroll
        for (int qt = 0; qt < 2; ++qt)
#pragma unroll
          for (int e = 0; e < 8; e += 2) {
            const f32x4 a = acc[qt][e], c = acc[qt][e + 1];
            __builtin_amdgcn_raw_buffer_store_b128(u32x4{pack_bf16(a[0], a[1]), pack_bf16(a[2], a[3]), pack_bf16(c[0], c[1]), pack_bf16(c[2], c[3])},
                                                   prs, lane * 16 + (e >> 1) * 1024 + qt * 4096, 0, kAuxSC1);
          }
        __builtin_amdgcn_raw_buffer_store_b128(lds_ld<u32x4>(ql + c16 * 16), prs, lane * 16 + 8192, 0, kAuxSC1);
        asm volatile("s_waitcnt vmcnt(0)" ::: "memory");
        if (lane == 0) __hip_atomic_store(wX, Ev + 1, __ATOMIC_RELAXED, __HIP_MEMORY_SCOPE_AGENT);
#pragma unroll
        for (int qt = 0; qt < 2; ++qt)
#pragma unroll
          for (int e = 0; e < 8; ++e) acc[qt][e] = f32x4{0.f, 0.f, 0.f, 0.f};
      } else {
        f32x2 st[2] = {lds_ld<f32x2>(mw), lds_ld<f32x2>(mw + 16 * kNW * 8)};
        u64 Xv = __hip_atomic_load(wX, __ATOMIC_RELAXED, __HIP_MEMORY_SCOPE_AGENT);
        E = rfl64(Ev);
        bool got;
        for (unsigned polls = 0;; ++polls) {
          got = rfl64(Xv) == E + 1;
          if (got || polls > kMaxPolls) break;
          __builtin_amdgcn_s_sleep(2);
          Xv = __hip_atomic_load(wX, __ATOMIC_RELAXED, __HIP_MEMORY_SCOPE_AGENT);
        }
        __builtin_amdgcn_fence(__ATOMIC_ACQUIRE, "agent");
        f32x4 pm = __builtin_bit_cast(f32x4, __builtin_amdgcn_raw_buffer_load_b128(prs, lane * 16, 8192, kAuxSC1));
        u32x4 pa[2][4];
#pragma unroll
        for (int qt = 0; qt < 2; ++qt)
#pragma unroll
          for (int u = 0; u < 4; ++u) pa[qt][u] = __builtin_amdgcn_raw_buffer_load_b128(prs, lane * 16 + u * 1024, qt * 4096, kAuxSC1);
        if (!got) pm[1] = pm[3] = __builtin_nanf("");
#pragma unroll
        for (int qt = 0; qt < 2; ++qt) {
          const float mn = fmaxf(st[qt][0], pm[2 * qt]);
          const float fa = __builtin_amdgcn_exp2f(st[qt][0] - mn), fb = __builtin_amdgcn_exp2f(pm[2 * qt] - mn);
          st[qt] = f32x2{mn, st[qt][1] * fa + pm[2 * qt + 1] * fb};
#pragma unroll
          for (int e = 0; e < 8; ++e) {
            const unsigned v0 = pa[qt][e >> 1][2 * (e & 1)], v1 = pa[qt][e >> 1][2 * (e & 1) + 1];
            acc[qt][e] = acc[qt][e] * fa + f32x4{bf_lo(v0), bf_hi(v0), bf_lo(v1), bf_hi(v1)} * fb;
          }
        }
        if (g4 == 0) { lds_st<f32x2>(mw, st[0]); lds_st<f32x2>(mw + 16 * kNW * 8, st[1]); }
      }
    }
#pragma unroll
    for (int e = 0; e < 8; ++e) park(e);
    asm volatile("" ::: "memory");
  }

  asm volatile("s_waitcnt lgkmcnt(0)" ::: "memory");
  __builtin_amdgcn_s_barrier();
  asm volatile("" ::: "memory");
  {
    if (lender) load_ml();
    f32x4 x0[kNW], x1[kNW];
#pragma unroll
    for (int w = 0; w < kNW; ++w) {
      x0[w] = *(LAS f32x4*)(uintptr_t)((w < 4 ? xa0 : xb0) + (w & 3) * kWaveLds);
      x1[w] = *(LAS f32x4*)(uintptr_t)((w < 4 ? xa1 : xb1) + (w & 3) * kWaveLds);
    }
    __builtin_amdgcn_sched_barrier(0);
    if (lender) weights();
    f32x4 s0 = fw[0] * x0[0], s1 = fw[0] * x1[0];
#pragma unroll
    for (int w = 1; w < kNW; ++w) { s0 += fw[w] * x0[w]; s1 += fw[w] * x1[w]; }
    __builtin_amdgcn_raw_buffer_store_b128(u32x4{pack_bf16(s0[0], s0[1]), pack_bf16(s0[2], s0[3]), pack_bf16(s1[0], s1[1]), pack_bf16(s1[2], s1[3])},
                                           make_rsrc((void*)optr, 32 * kD * 2), oo, 0, kAuxSC1);
  }
  if (taker && lane == 0) __hip_atomic_store(wE, E + 1, __ATOMIC_RELAXED, __HIP_MEMORY_SCOPE_AGENT);
  asm volatile(".Lpaged_attn_end:");
}

}

extern "C" void kernel_launch(void* const* d_in, const int* in_sizes, int n_in, void* d_out, int out_size, void* d_ws,
                              size_t ws_size, hipStream_t stream) {
  const unsigned kv_bytes = (unsigned)in_sizes[1] * 2u;
  const unsigned pace = kPace;
  (void)hipFuncSetAttribute((const void*)paged_attn_kernel, hipFuncAttributeMaxDynamicSharedMemorySize, kLdsBytes);
  hipLaunchKernelGGL(paged_attn_kernel, dim3(kB * kHKV), dim3(kNW * 64), kLdsBytes, stream, (const void*)d_in[0], (const void*)d_in[1],
                     (const void*)d_in[2], (const void*)d_in[3], (const void*)d_in[4], d_out, d_ws, kv_bytes, pace);
}
```
